```python
import torch

def _softmax(x, axis=-1):
    return torch.softmax(x, dim=axis)
def _gelu(x):
    return torch.nn.functional.gelu(x, approximate="tanh")
def _silu(x):
    return torch.nn.functional.silu(x)
def _sigmoid(x):
    return torch.sigmoid(x)
def _layernorm(x, axis=-1, eps=1e-5):
    m = x.mean(dim=axis, keepdim=True)
    v = x.var(dim=axis, keepdim=True, unbiased=False)
    return (x - m) / torch.sqrt(v + eps)
def _scatter_add(tgt, idx, src, axis=0):
    return tgt.clone().index_add_(axis, idx, src)
def _segment_sum(src, seg, n):
    out = torch.zeros((n,) + src.shape[1:], dtype=src.dtype)
    return out.index_add_(0, seg, src)
def _pad(x, pw):
    flat = tuple(v for pair in reversed(pw) for v in pair)
    return torch.nn.functional.pad(x, flat)

kernel_name = 'sequential_preLN__ssm_conv_s4d__moe_arch_mod__prompt'

def setup_inputs():

    _w = (lambda a: (lambda t: t.to(torch.bfloat16) if t.is_floating_point() else t)(torch.as_tensor(a)))
    def _A_setup():
        Bsz, L, D, N = 1, 2048, 640, 128
        return {
            "u": _w((lambda *s: torch.randn(*s, dtype=torch.bfloat16) * 0.05)(Bsz, L, D)),
            "log_A_real": _w((lambda *s: torch.randn(*s, dtype=torch.bfloat16) * 6.4)(D, N) * 0.5),
            "A_imag": _w((lambda *s: torch.randn(*s, dtype=torch.bfloat16) * 0.05)(D, N) * 3.14),
            "C_real": _w((lambda *s: torch.randn(*s, dtype=torch.bfloat16) * 0.05)(D, N) / N ** 0.5),
            "C_imag": _w((lambda *s: torch.randn(*s, dtype=torch.bfloat16) * 0.05)(D, N) / N ** 0.5),
            "log_dt": _w((lambda *s: torch.randn(*s, dtype=torch.bfloat16) * 0.05)(D) * 0.5 - 4.0),
            "D_skip": _w((lambda *s: torch.randn(*s, dtype=torch.bfloat16) * 0.05)(D)),
        }
    _A_in = _A_setup()

    def _B_setup():
        B, S, D, F, C = 1, 2048, 640, 1728, 1
        return {
            "x": _w((lambda *s: torch.randn(*s, dtype=torch.bfloat16) * 0.05)(B, S, D)),
            "w_r": _w((lambda *s: torch.randn(*s, dtype=torch.bfloat16) * 0.05)(D)),
            "w_gate": _w((lambda *s: torch.randn(*s, dtype=torch.bfloat16) * 0.05)(D, F)),
            "w_up": _w((lambda *s: torch.randn(*s, dtype=torch.bfloat16) * 0.05)(D, F)),
            "w_down": _w((lambda *s: torch.randn(*s, dtype=torch.bfloat16) * 0.05)(F, D)),
        }
    _B_in = _B_setup()

    return {
        "A_u": _A_in["u"],
        "A_log_A_real": _A_in["log_A_real"],
        "A_A_imag": _A_in["A_imag"],
        "A_C_real": _A_in["C_real"],
        "A_C_imag": _A_in["C_imag"],
        "A_log_dt": _A_in["log_dt"],
        "A_D_skip": _A_in["D_skip"],
        "B_x": _B_in["x"],
        "B_w_r": _B_in["w_r"],
        "B_w_gate": _B_in["w_gate"],
        "B_w_up": _B_in["w_up"],
        "B_w_down": _B_in["w_down"],
    }

def reference(A_u, A_log_A_real, A_A_imag, A_C_real, A_C_imag, A_log_dt, A_D_skip, B_x, B_w_r, B_w_gate, B_w_up, B_w_down):
    def _A_ref(u, log_A_real, A_imag, C_real, C_imag, log_dt, D_skip):
        L = u.shape[1]
        dt = torch.exp(log_dt)
        A = -torch.exp(log_A_real) + 1j * A_imag
        C = (C_real + 1j * C_imag) * dt[:, None]
        pos = (lambda x, dt: x.to(dt))(torch.arange(L), log_dt.dtype)
        P = torch.exp((A * dt[:, None])[:, :, None] * pos[None, None, :])
        K = 2.0 * torch.einsum("dn,dnl->dl", C, P).real
        L2 = 2 * L
        u_t = (lambda x, *d: x.permute(*d))(u, 0, 2, 1)
        Kf = (lambda x, *a, **k: torch.fft.rfft(x.float(), *a, **k))(K, L2)
        Uf = (lambda x, *a, **k: torch.fft.rfft(x.float(), *a, **k))(u_t, L2)
        y = (lambda x, *a, **k: torch.fft.irfft(x, *a, **k).to(torch.bfloat16))(Uf * Kf[None, :, :], L2)[:, :, :L]
        y = (lambda x, *d: x.permute(*d))(y, 0, 2, 1)
        return y + u * D_skip[None, None, :]

    def _B_ref(x, w_r, w_gate, w_up, w_down):
        B, S, D = x.shape
        C = 1
        r = x @ w_r
        idx = torch.argsort(-r, axis=-1)[:, :C]
        idx_e = (lambda x, n, axis: torch.repeat_interleave(x, n, dim=axis))(idx.reshape(B, C, 1), D, axis=2)
        x_sel = (lambda x, i, axis: torch.take_along_dim(x, i, dim=axis))(x, idx_e, axis=1)
        g = _silu(x_sel @ w_gate)
        u = x_sel @ w_up
        y = (g * u) @ w_down
        r_sel = _sigmoid((lambda x, i, axis: torch.take_along_dim(x, i, dim=axis))(r, idx, axis=1))
        y = y * r_sel.reshape(B, C, 1)
        b_off = (torch.arange(B) * S).reshape(B, 1)
        flat_idx = (idx + b_off).reshape(B * C)
        zero = (x * 0.0).reshape(B * S, D)
        upd = _scatter_add(zero, flat_idx, y.reshape(B * C, D), axis=0)
        return x + upd.reshape(B, S, D)

    x = A_u
    h = x + _A_ref(_layernorm(x, axis=-1), A_log_A_real, A_A_imag, A_C_real, A_C_imag, A_log_dt, A_D_skip)
    h2 = h + _B_ref(_layernorm(h, axis=-1), B_w_r, B_w_gate, B_w_up, B_w_down)
    return h2
```

```cpp
#include <hip/hip_runtime.h>
#include <stdint.h>
#include <utility>

typedef unsigned u32;
typedef unsigned short u16;
typedef unsigned long long u64;
typedef u32 v4u __attribute__((ext_vector_type(4)));
typedef u32 v2u __attribute__((ext_vector_type(2)));
typedef float v4f __attribute__((ext_vector_type(4)));
typedef float v2f __attribute__((ext_vector_type(2)));
typedef _Float16 v8h __attribute__((ext_vector_type(8)));
typedef _Float16 v2h __attribute__((ext_vector_type(2)));
typedef __bf16 v2bf __attribute__((ext_vector_type(2)));
typedef short v4s __attribute__((ext_vector_type(4)));
#define LAS __attribute__((address_space(3)))
#define GAS __attribute__((address_space(1)))
#define DEVI __device__ __forceinline__

#define WS_EPOCH 0u
#define WS_UG 4096u
#define WS_YG (WS_UG + 256u * 10240u)
#define WS_KEY (WS_YG + 32u * 640u * 128u)
#define WS_XS (WS_KEY + 256u * 128u)
#define WS_ACT (WS_XS + 256u * 5120u)
#define WS_PFL (WS_ACT + 256u * 128u)
#define WS_END (WS_PFL + 64u * 128u)
#define WS_OOB 0x7ffffff0u
#define TAGM 0x40074007u

#define L_CNTK 16
#define L_CNTR 0x40
#define L_XCH (L_TAB + 8 * TAB_W)
#define L_INVS 96
#define L_TAG 112
#define L_ROWKEY 128
#define L_WKEY 192
#define L_RED 1024
#define L_KC 4096
#define KC_CP 2368
#define KC_Q (4 * KC_CP)
#define KC_CH 10752
#define L_U 36352
#define U_CH 4656
#define L_STG 50432
#define STG1_RS 1312
#define STG2_RS 2592
#define L_TAB 60928
#define TAB_W 4608
#define TAB_RS 288
#define L_TOTAL (L_XCH + 4 * 3072)

#define CODE_BYTES 26624
#define SPIN_MAX 200000

#define OUT_AUX 0
#define WPF 1
#define PFW 1
#define TLBW 0
#define D_PFL 28
#define PACE 0
#define D_W 4
#define D_U 20
#define D_Y 16
#define D_KEY 14
#define D_ACT 33

#define TS(i) do { } while (0)
#define TC(i) do { } while (0)
#define PIN(x) do { } while (0)
#define TSARG
#define TSPASS

#define TP(i) do { } while (0)
DEVI float lo2f(u32 v) { return __uint_as_float(v << 16); }
DEVI float hi2f(u32 v) { return __uint_as_float(v & 0xffff0000u); }
DEVI u32 pkbf(float a, float b) { v2f t = {a, b}; return __builtin_bit_cast(u32, __builtin_convertvector(t, v2bf)); }
DEVI u32 pkh(float a, float b) { v2f t = {a, b}; return __builtin_bit_cast(u32, __builtin_convertvector(t, v2h)); }
DEVI u32 pkh2(v2f t) { return __builtin_bit_cast(u32, __builtin_convertvector(t, v2h)); }
DEVI float rbf(float a) { return lo2f(pkbf(a, a)); }
DEVI float dot2bf(u32 a, u32 b, float c) {
    return __builtin_amdgcn_fdot2_f32_bf16(__builtin_bit_cast(v2bf, a), __builtin_bit_cast(v2bf, b), c, false);
}
template <int CTRL, int RM, int BM>
DEVI float dpp0(float v) {
    return __builtin_bit_cast(float, __builtin_amdgcn_update_dpp(0, __builtin_bit_cast(int, v), CTRL, RM, BM, true));
}
DEVI float wave_sum(float v) {
    v += dpp0<0x111, 0xf, 0xf>(v);
    v += dpp0<0x112, 0xf, 0xf>(v);
    v += dpp0<0x114, 0xf, 0xf>(v);
    v += dpp0<0x118, 0xf, 0xf>(v);
    v += dpp0<0x142, 0xa, 0xf>(v);
    v += dpp0<0x143, 0xc, 0xf>(v);
    return __builtin_bit_cast(float, __builtin_amdgcn_readlane(__builtin_bit_cast(int, v), 63));
}
template <int CTRL, int RM, int BM>
DEVI u32 dppu0(u32 v) { return (u32)__builtin_amdgcn_update_dpp(0, (int)v, CTRL, RM, BM, true); }
DEVI u32 wave_maxu(u32 v) {
    v = max(v, dppu0<0x111, 0xf, 0xf>(v));
    v = max(v, dppu0<0x112, 0xf, 0xf>(v));
    v = max(v, dppu0<0x114, 0xf, 0xf>(v));
    v = max(v, dppu0<0x118, 0xf, 0xf>(v));
    v = max(v, dppu0<0x142, 0xa, 0xf>(v));
    v = max(v, dppu0<0x143, 0xc, 0xf>(v));
    return (u32)__builtin_amdgcn_readlane((int)v, 63);
}
#define DPPADD(r, ctl) "v_add_f32_dpp " r ", " r ", " r " " ctl "\n\t"
#define DPP_S1 "row_shr:1 row_mask:0xf bank_mask:0xf bound_ctrl:1"
#define DPP_S2 "row_shr:2 row_mask:0xf bank_mask:0xf bound_ctrl:1"
#define DPP_S4 "row_shr:4 row_mask:0xf bank_mask:0xf bound_ctrl:1"
#define DPP_S8 "row_shr:8 row_mask:0xf bank_mask:0xf bound_ctrl:1"
#define DPP_B15 "row_bcast:15 row_mask:0xa bank_mask:0xf"
#define DPP_B31 "row_bcast:31 row_mask:0xc bank_mask:0xf"
DEVI float rl63(float v) { return __builtin_bit_cast(float, __builtin_amdgcn_readlane(__builtin_bit_cast(int, v), 63)); }
DEVI void wave_sum2(float& a, float& b) {
#define ST2(c) DPPADD("%0", c) DPPADD("%1", c) "s_nop 0\n\t"
    asm("s_nop 1\n\t" ST2(DPP_S1) ST2(DPP_S2) ST2(DPP_S4) ST2(DPP_S8) ST2(DPP_B15) ST2(DPP_B31) : "+v"(a), "+v"(b));
    a = rl63(a); b = rl63(b);
}
DEVI void wave_sum3(float& a, float& b, float& c) {
#define ST3(c) DPPADD("%0", c) DPPADD("%1", c) DPPADD("%2", c)
    asm("s_nop 1\n\t" ST3(DPP_S1) ST3(DPP_S2) ST3(DPP_S4) ST3(DPP_S8) ST3(DPP_B15) ST3(DPP_B31) "s_nop 0" : "+v"(a), "+v"(b), "+v"(c));
    a = rl63(a); b = rl63(b); c = rl63(c);
}
DEVI v2f unpk(u32 v) { v2f r = {lo2f(v), hi2f(v)}; return r; }
DEVI u32 pkbf2(v2f t) { return __builtin_bit_cast(u32, __builtin_convertvector(t, v2bf)); }
DEVI float rfl(float v) { return __builtin_bit_cast(float, __builtin_amdgcn_readfirstlane(__builtin_bit_cast(int, v))); }
DEVI v2f cmul(v2f a, v2f b) {
    v2f r;
    asm("v_pk_mul_f32 %0, %1, %2 op_sel_hi:[1,0]\n\t"
        "v_pk_fma_f32 %0, %1, %2, %0 op_sel:[1,1,0] op_sel_hi:[0,1,1] neg_lo:[1,0,0]" : "=&v"(r) : "v"(a), "v"(b));
    return r;
}
template <int O0, int O1>
DEVI void tw2(v2f a, v2f b) {
    asm volatile("ds_write_addtid_b32 %0 offset:%2\n\tds_write_addtid_b32 %1 offset:%3" :: "v"(pkh2(a)), "v"(pkh2(b)), "i"(O0), "i"(O1) : "memory");
}
template <int O0, int O1, int O2>
DEVI void tw3(v2f a, v2f b, v2f c) {
    asm volatile("ds_write_addtid_b32 %0 offset:%3\n\tds_write_addtid_b32 %1 offset:%4\n\tds_write_addtid_b32 %2 offset:%5"
                 :: "v"(pkh2(a)), "v"(pkh2(b)), "v"(pkh2(c)), "i"(O0), "i"(O1), "i"(O2) : "memory");
}
template <int O0, int O1, int O2, int O3>
DEVI void tw4(v2f a, v2f b, v2f c, v2f d) {
    asm volatile("ds_write_addtid_b32 %0 offset:%4\n\tds_write_addtid_b32 %1 offset:%5\n\tds_write_addtid_b32 %2 offset:%6\n\tds_write_addtid_b32 %3 offset:%7"
                 :: "v"(pkh2(a)), "v"(pkh2(b)), "v"(pkh2(c)), "v"(pkh2(d)), "i"(O0), "i"(O1), "i"(O2), "i"(O3) : "memory");
}
DEVI v2f zpow(float lam, float om, float p) {
    float e = __builtin_amdgcn_exp2f(lam * p);
    float t = __builtin_amdgcn_fractf(om * p);
    v2f r = {e * __builtin_amdgcn_cosf(t), e * __builtin_amdgcn_sinf(t)};
    return r;
}
#define LDS32(off) (*(LAS u32*)(uintptr_t)(off))
#define LDSF(off) (*(LAS float*)(uintptr_t)(off))
#define LDS64(off) (*(LAS v2u*)(uintptr_t)(off))
#define LDS128(off) (*(LAS v4u*)(uintptr_t)(off))
#define LDSF4(off) (*(LAS v4f*)(uintptr_t)(off))
#define BAR() asm volatile("s_waitcnt lgkmcnt(0)\n\ts_barrier" ::: "memory")
#define CBAR() asm volatile("" ::: "memory")
DEVI void lds_arrive(u32 off) {
    asm volatile("s_mov_b64 exec, 1\n\tds_add_u32 %0, %1\n\ts_mov_b64 exec, -1" :: "v"(off), "v"(1u) : "memory");
}
template <int SLP>
DEVI void lds_wait(u32 off, u32 n) {
#pragma unroll 1
    for (int s = 0; s < (1 << 20); ++s) {
        u32 v = __hip_atomic_load((LAS u32*)(uintptr_t)off, __ATOMIC_RELAXED, __HIP_MEMORY_SCOPE_WORKGROUP);
        if ((u32)__builtin_amdgcn_readfirstlane((int)v) >= n) break;
        if (SLP) __builtin_amdgcn_s_sleep(SLP);
    }
    CBAR();
}
template <int N>
DEVI void sleep_n() {
    if (N > 0) __builtin_amdgcn_s_sleep(N);
}
#define LDW4(off) __builtin_amdgcn_raw_buffer_load_b128(rw, (off), 0, 16)
#define LDW2(off) __builtin_amdgcn_raw_buffer_load_b64(rw, (off), 0, 16)
#define STW4(v, off) __builtin_amdgcn_raw_buffer_store_b128((v), rw, (off), 0, 16)
#define STW2(v, off) __builtin_amdgcn_raw_buffer_store_b64((v), rw, (off), 0, 16)

DEVI v4f mfma16(v4u a, v4u b, v4f c) {
    return __builtin_amdgcn_mfma_f32_16x16x32_f16(__builtin_bit_cast(v8h, a), __builtin_bit_cast(v8h, b), c, 0, 0, 0);
}

DEVI void gather3(__amdgpu_buffer_rsrc_t rw, u32 tag, u32 o0, u32 o1, u32 o2, v4u& a0, v4u& a1, v4u& a2) {
    a0 = LDW4(o0); a1 = LDW4(o1); a2 = LDW4(o2);
#pragma unroll 1
    for (int s = 0; s < SPIN_MAX; ++s) {
        if (a0.y == tag && a0.w == tag) o0 = WS_OOB;
        if (a1.y == tag && a1.w == tag) o1 = WS_OOB;
        if (a2.y == tag && a2.w == tag) o2 = WS_OOB;
        if (__all((o0 & o1 & o2) == WS_OOB)) break;
        __builtin_amdgcn_s_sleep(2);
        CBAR();
        v4u n0 = LDW4(o0), n1 = LDW4(o1), n2 = LDW4(o2);
        if (o0 != WS_OOB) a0 = n0;
        if (o1 != WS_OOB) a1 = n1;
        if (o2 != WS_OOB) a2 = n2;
    }
}
template <int MODE>
DEVI bool chunk_ok(v4u& a, const v4u& T) {
    if (MODE == 0) { a ^= T; return ((a.x | a.y | a.z | a.w) & TAGM) == 0u; }
    return max(max(a.x, a.y), max(a.z, a.w)) != 0xffffffffu;
}
template <int MODE>
DEVI void gather2_fin(__amdgpu_buffer_rsrc_t rw, const v4u& T, u32 o0, u32 o1, v4u& a0, v4u& a1) {
    const bool k0 = chunk_ok<MODE>(a0, T), k1 = chunk_ok<MODE>(a1, T) || o1 == WS_OOB;
    if (__builtin_expect(__all(k0 && k1), 1)) return;
    if (k0) o0 = WS_OOB;
    if (k1) o1 = WS_OOB;
#pragma unroll 1
    for (int s = 0; s < SPIN_MAX; ++s) {
        __builtin_amdgcn_s_sleep(2);
        CBAR();
        v4u n0 = LDW4(o0), n1 = LDW4(o1);
        if (o0 != WS_OOB) { a0 = n0; if (chunk_ok<MODE>(a0, T)) o0 = WS_OOB; }
        if (o1 != WS_OOB) { a1 = n1; if (chunk_ok<MODE>(a1, T)) o1 = WS_OOB; }
        if (__all((o0 & o1) == WS_OOB)) break;
    }
}
DEVI v4u gather1(__amdgpu_buffer_rsrc_t rw, u32 tag, u32 o0) {
    v4u a0 = LDW4(o0);
#pragma unroll 1
    for (int s = 0; s < SPIN_MAX; ++s) {
        if (a0.y == tag && a0.w == tag) o0 = WS_OOB;
        if (__all(o0 == WS_OOB)) break;
        __builtin_amdgcn_s_sleep(2);
        CBAR();
        v4u n0 = LDW4(o0);
        if (o0 != WS_OOB) a0 = n0;
    }
    return a0;
}
DEVI u32 gather1h(__amdgpu_buffer_rsrc_t rw, u32 tag, u32 o0) {
    v2u a0 = LDW2(o0);
#pragma unroll 1
    for (int s = 0; s < SPIN_MAX; ++s) {
        if (a0.y == tag) o0 = WS_OOB;
        if (__all(o0 == WS_OOB)) break;
        __builtin_amdgcn_s_sleep(2);
        CBAR();
        v2u n0 = LDW2(o0);
        if (o0 != WS_OOB) a0 = n0;
    }
    return a0.x;
}

template <int OFF>
DEVI void lda(v4u& d, u32 addr) { asm volatile("ds_read_b128 %0, %1 offset:%2" : "=v"(d) : "v"(addr), "i"(OFF)); }
template <int OFF>
DEVI void ldh(v2u& d, u32 addr) { asm volatile("ds_read_b64 %0, %1 offset:%2" : "=v"(d) : "v"(addr), "i"(OFF)); }
template <int N, class F, int... I>
DEVI void sfor_impl(F&& f, std::integer_sequence<int, I...>) { (f(std::integral_constant<int, I>{}), ...); }
template <int N, class F>
DEVI void sfor(F&& f) { sfor_impl<N>(f, std::make_integer_sequence<int, N>{}); }
DEVI v4u cat(v2u lo, v2u hi) { v4u r = {lo.x, lo.y, hi.x, hi.y}; return r; }

#define SB() __builtin_amdgcn_sched_barrier(0)
#define KEEP(x) asm volatile("" : "+v"(x))
#define CDIST 9
template <int TH, int N, int NCH>
struct Sched {
    int M, L;
    int mk[160], mc[160], mx[160], my[160], ma[160];
    int lk[160], lc[160], lx[160], ls[160];
    int l0[162], ln[162];
    constexpr void addm(int k, int c, int x, int y, int a) { mk[M] = k; mc[M] = c; mx[M] = x; my[M] = y; ma[M] = a; ++M; }
    constexpr void need(int k, int c, int x, int at, int* nd) {
        for (int i = 0; i < L; ++i) if (lk[i] == k && lc[i] == c && lx[i] == x) return;
        lk[L] = k; lc[L] = c; lx[L] = x; nd[L] = at; ++L;
    }
    constexpr Sched() : M(0), L(0), mk{}, mc{}, mx{}, my{}, ma{}, lk{}, lc{}, lx{}, ls{}, l0{}, ln{} {
        const int NT = TH >= 4 ? TH - 3 : 0;
        for (int t = 0; t < NT; ++t) { addm(1, 0, t, 0, 0); addm(2, 0, t, 0, 0); }
        for (int c = 0; c < 8; ++c) {
            for (int ch = 0; ch < NCH; ++ch)
                for (int b = TH; b >= 0; --b)
                    for (int a = 0; a < 4; ++a)
                        if (a + b <= TH && a + b > TH - N) addm(0, ch, 8 * a + c, 8 * b - c + 7, TH - a - b);
            if (c == 1) for (int t = 0; t < NT; ++t) for (int m = 0; m < N; ++m) if (TH - m - 4 - t >= 0) addm(3, 0, t, TH - m - 4 - t, m);
            if (c == 3 && NT > 0) for (int m = 0; m < N; ++m) addm(0, 0, 32, 8 * (TH - m) - 32 + 7 + 64, m);
        }
        int nd[160] = {};
        for (int i = 0; i < M; ++i) {
            if (mk[i] == 0) { need(2, mc[i], my[i], i, nd); need(0, mc[i], mx[i], i, nd); need(1, mc[i], mx[i], i, nd); }
            else if (mk[i] == 3) need(5, 0, my[i], i, nd);
            else { need(3, 0, mx[i], i, nd); need(4, 0, mx[i], i, nd); }
        }
        const int cap = L > M ? 2 : 1;
        int slot = M, used = cap;
        for (int i = L - 1; i >= 0; --i) {
            int want = nd[i] - CDIST;
            if (want < slot) { slot = want; used = 0; }
            else if (used >= cap) { --slot; used = 0; }
            ++used;
            ls[i] = slot < 0 ? -1 : slot;
            l0[ls[i] + 1] = i; ++ln[ls[i] + 1];
        }
    }
};
DEVI v2u pk4h(v4f d) { v2u r = {pkh(d[0], d[1]), pkh(d[2], d[3])}; return r; }

template <int TH, int N, int NCH>
DEVI void conv_job(__amdgpu_buffer_rsrc_t rw, u32 invad, u32 aad, u32 bad, u32 qad, u32 yoff, v4u bx, v4u by TSARG) {
    constexpr Sched<TH, N, NCH> S{};
    v2u AL[NCH][33], AH[NCH][33], FL[4], FH[4], VX[4], VY[4];
    v4u B[NCH][128], Q[4];
    v4f acc[NCH][N], W[8];
    const v4f zero4 = {0.f, 0.f, 0.f, 0.f};
#pragma unroll
    for (int c = 0; c < NCH; ++c)
#pragma unroll
        for (int m = 0; m < N; ++m) acc[c][m] = zero4;
    auto loads = [&](auto islot) {
        sfor<S.ln[islot.value]>([&](auto il) {
            constexpr int i = S.l0[decltype(islot)::value] + il.value;
            {
                constexpr int k = S.lk[i], c = S.lc[i], x = S.lx[i];
                if constexpr (k == 0) AL[c][x] = LDS64(aad + (32 - x) * 64 + c * 2 * KC_CH);
                if constexpr (k == 1) AH[c][x] = LDS64(aad + (32 - x) * 64 + 8 + c * 2 * KC_CH);
                if constexpr (k == 2) B[c][x] = LDS128(bad + 8 + (x & 63) * 64 + c * 2 * U_CH);
                if constexpr (k == 3) FL[x] = LDS64(bad + 448 + x * 512);
                if constexpr (k == 4) FH[x] = LDS64(bad + 448 + x * 512 + 8);
                if constexpr (k == 5) Q[x] = LDS128(qad + x * 256);
                SB();
            }
        });
    };
    loads(std::integral_constant<int, 0>{});
    float inv[NCH];
    sfor<S.M>([&](auto im) {
        constexpr int i = im.value, k = S.mk[i], c = S.mc[i], x = S.mx[i], y = S.my[i], a = S.ma[i];
        if constexpr (k == 0) acc[c][a] = mfma16(cat(AL[c][x], AH[c][x]), B[c][y], acc[c][a]);
        if constexpr (k == 1) W[2 * x] = mfma16(cat(FL[x], FH[x]), bx, zero4);
        if constexpr (k == 2) W[2 * x + 1] = mfma16(cat(FL[x], FH[x]), by, zero4);
        if constexpr (k == 3) acc[0][a] = mfma16(cat(VX[x], VY[x]), Q[y], acc[0][a]);
        SB();
        if constexpr (i >= 2 && S.mk[i >= 2 ? i - 2 : 0] == 1) { VX[S.mx[i >= 2 ? i - 2 : 0]] = pk4h(W[2 * S.mx[i >= 2 ? i - 2 : 0]]); KEEP(VX[S.mx[i >= 2 ? i - 2 : 0]]); SB(); }
        if constexpr (i >= 2 && S.mk[i >= 2 ? i - 2 : 0] == 2) { VY[S.mx[i >= 2 ? i - 2 : 0]] = pk4h(W[2 * S.mx[i >= 2 ? i - 2 : 0] + 1]); KEEP(VY[S.mx[i >= 2 ? i - 2 : 0]]); SB(); }
        loads(std::integral_constant<int, i + 1>{});
        if constexpr (i == S.M - 6) {
#pragma unroll
            for (int cc = 0; cc < NCH; ++cc) inv[cc] = LDSF(invad + cc * 8);
            SB();
        }
    });
    PIN(acc[0][0]);
    TC(11);
    __builtin_amdgcn_s_setprio(3);
#pragma unroll
    for (int c = 0; c < NCH; ++c)
#pragma unroll
        for (int m = 0; m < N; m += 2) {
            const v4f p = acc[c][m] * inv[c], q = acc[c][m + 1] * inv[c];
            const u32 a0 = min(pkbf(p[0], p[1]), 0xfffffffeu), a1 = min(pkbf(p[2], p[3]), 0xfffffffeu);
            const u32 b0 = min(pkbf(q[0], q[1]), 0xfffffffeu), b1 = min(pkbf(q[2], q[3]), 0xfffffffeu);
            const auto s0 = __builtin_amdgcn_permlane16_swap(a0, b0, false, false);
            const auto s1 = __builtin_amdgcn_permlane16_swap(a1, b1, false, false);
            v4u o = {s0[0], s1[0], s0[1], s1[1]};
            STW4(o, yoff + (u32)(TH - m) * (4u * 640u * 128u) + c * 256);
        }
}

DEVI float rowreduce8(float (&v)[8], int lane) {
    float a[4], b[2];
    const bool b3 = lane & 8, b2 = lane & 4, b1 = lane & 2;
#pragma unroll
    for (int i = 0; i < 4; ++i) { float k = b3 ? v[i + 4] : v[i], s = b3 ? v[i] : v[i + 4]; a[i] = k + dpp0<0x140, 0xf, 0xf>(s); }
#pragma unroll
    for (int i = 0; i < 2; ++i) { float k = b2 ? a[i + 2] : a[i], s = b2 ? a[i] : a[i + 2]; b[i] = k + dpp0<0x141, 0xf, 0xf>(s); }
    float k = b1 ? b[1] : b[0], s = b1 ? b[0] : b[1];
    return k + dpp0<0x1B, 0xf, 0xf>(s);
}

struct KTab {
    u32 v[5][64][4];
    constexpr u32& at(int lane, int i) { return v[i >> 2][lane][i & 3]; }
    constexpr KTab() : v{} {
        for (int lane = 0; lane < 64; ++lane) {
            const int bb = lane & 15, g = lane >> 4, sg = (3 - bb) & 3;
            for (int j = 0; j < 8; ++j) {
                const int d = 8 * g + j - bb;
                if (d >= 0 && d <= 8) at(lane, j >> 1) |= 0x4000u << (16 * (j & 1));
                if (d >= 9 && d <= 15) at(lane, 4 + (j >> 1)) |= 0x4000u << (16 * (j & 1));
            }
            at(lane, 8) = (u32)(L_KC + 16 + sg * KC_CP + (1039 - bb - sg + 8 * g) * 2 - 64 * 32);
            at(lane, 9) = (u32)(L_U + (16 * bb + 8 * g) * 2 - 8);
            at(lane, 10) = (u32)(L_KC + KC_Q + (bb - 4 * g + 12) * 16);
            at(lane, 11) = (u32)((int)WS_YG + (2 * (bb >> 3) + (g >> 1) - 4 * (g & 1)) * 640 * 128 + (bb & 7) * 16);
            for (int hh = 0; hh < 2; ++hh) {
                at(lane, 12 + 4 * hh) = (u32)(L_KC + 8 + (hh ? (1048 - (512 + 128 * g + 8 * bb)) : (1052 - 256 - (128 * (g >> 1) + 8 * bb + 4 * (g & 1)))) * 2);
                at(lane, 13 + 4 * hh) = (u32)(L_KC + KC_Q + (8 * ((hh ? 0 : 4) + g) + (bb >> 1) + 12) * 16 + ((bb & 1) ? 0 : 8));
                int z = 0;
                if (hh == 0) {
                    at(lane, 14) = lane < 12 ? KC_CH : U_CH;
                    z = lane < 12 ? L_KC + (lane / 3) * KC_CP + 2112 + (lane % 3) * 16 : L_U + (lane < 42 ? lane - 12 : 0) * 16;
                } else {
                    at(lane, 18) = (u32)(L_KC + (lane < 20 ? 16 + (lane & 3) * KC_CP + (31 - (lane >> 2) - (lane & 3)) * 2 : 2208));
                    z = lane < 16 ? L_KC + (lane >> 2) * KC_CP + 16 + (lane & 3) * 16 : L_KC + KC_Q + (lane < 31 ? lane - 16 : 0) * 16;
                }
                at(lane, 15 + 4 * hh) = (u32)z;
            }
        }
    }
};
__device__ const __attribute__((aligned(128))) KTab g_ktab{};

DEVI float exp_bf(float x) {
    const float t = x * 1.44269504088896341f;
    const float lo = fmaf(x, 1.44269504088896341f, -t) + x * 1.925963033500011e-8f;
    const float e = __builtin_amdgcn_exp2f(t);
    return fmaf(e, lo * 0.6931471805599453f, e);
}

typedef u64 v8q __attribute__((ext_vector_type(8)));
typedef u64 v4q __attribute__((ext_vector_type(4)));
__global__ void __launch_bounds__(1024) fused_s4d_mod(
    const u16* a_x, const u16* a_logA, const u16* a_Aim, const u16* a_Cre, const u16* a_Cim, const u16* a_logdt,
    const u16* a_dsk, const u16* a_wr, const u16* a_wg, const u16* a_wu, const u16* a_wd, u16* a_out, char* a_ws) {
    v8q ka0; v4q ka1; u64 ka2;
    asm volatile("s_load_dwordx16 %0, %3, 0x0\n\ts_load_dwordx8 %1, %3, 0x40\n\ts_load_dwordx2 %2, %3, 0x60"
                 : "=&s"(ka0), "=&s"(ka1), "=&s"(ka2) : "s"(__builtin_amdgcn_kernarg_segment_ptr()));
    __shared__ __attribute__((aligned(256))) char smem[L_TOTAL];
    asm volatile("" :: "v"((LAS char*)smem) : "memory");
    u64 pc0;
    asm volatile("s_getpc_b64 %0" : "=s"(pc0));
    TS(0);
    const int tid = threadIdx.x, lane = tid & 63;
    const int w = __builtin_amdgcn_readfirstlane(tid >> 6);
    const int b = blockIdx.x;
    const int xj = b >> 3, xc = b & 7;
    u32 pf = 0;
    if (w == 15) {
        LDS32(lane * 4) = 0u;
        pf = *(const GAS u32*)((pc0 & ~127ull) + min(xj * 8 + (lane & 7), CODE_BYTES / 128 - 1) * 128);
    }
    const int cw = xc * 32 + xj;
    const int odd = cw & 1;
    const int ch0 = 5 * (cw >> 1) + 2 * odd;
    const bool is_gu = xj < 27;
    const int fg = xc * 27 + xj;
    const int dg = xc * 5 + (xj - 27);
    const int k = w - 8;
    const int slot = k < 4 ? k >> 1 : 2, hh = k & 1;
    const int row = cw * 8 + (w & 7);
    const bool isR = w < 8;
    const int chk = ch0 + slot;
    int so_r = row * 1280, so_k = chk * 256, so_t = (3 + hh) * 1024;
    asm volatile("" : "+s"(so_r), "+s"(so_k), "+s"(so_t));
    u32 io5 = lane * 16 + (isR ? so_r : so_t);
    u32 io0 = lane * 4 + (isR ? so_r + 1024 : so_k);
    u32 io1 = isR ? 0 : so_k >> 7;
    u32 io2 = lane * 4 + (isR ? 0 : so_k);
    u32 io3 = lane * 4 + (isR ? 1024 : so_k);
    const char* ktb = (const char*)&g_ktab;
    KEEP(io5); KEEP(io0); KEEP(io1); KEEP(io2); KEEP(io3);
    asm volatile("" : "+s"(ktb));
    asm volatile("s_waitcnt lgkmcnt(0)\n\ts_barrier" : "+s"(ka0), "+s"(ka1), "+s"(ka2) :: "memory");
    TP(21);
    const u16* const x = (const u16*)ka0[0]; const u16* const logA = (const u16*)ka0[1]; const u16* const Aim = (const u16*)ka0[2];
    const u16* const Cre = (const u16*)ka0[3]; const u16* const Cim = (const u16*)ka0[4]; const u16* const logdt = (const u16*)ka0[5];
    const u16* const dsk = (const u16*)ka0[6]; const u16* const wr = (const u16*)ka0[7];
    const u16* const wg = (const u16*)ka1[0]; const u16* const wu = (const u16*)ka1[1]; const u16* const wd = (const u16*)ka1[2];
    u16* const out = (u16*)ka1[3]; char* const ws = (char*)ka2;
    v4u in4 = *(const GAS v4u*)((isR ? (const char*)x : ktb) + io5);
    u32 in0 = *(const GAS u32*)((isR ? (const char*)x : (const char*)logA) + io0);
    u32 in1 = *(const GAS u16*)((const char*)logdt + io1);
    u32 in2 = __hip_atomic_load((const GAS u32*)((isR ? (const char*)ws : (const char*)Aim) + io2), __ATOMIC_RELAXED, __HIP_MEMORY_SCOPE_AGENT);
    u32 in3 = *(const GAS u32*)((isR ? (const char*)dsk : (const char*)Cre) + io3);
    u32 in5 = *(const GAS u32*)((isR ? (const char*)wr : (const char*)Cim) + io3);
    CBAR();
    __amdgpu_buffer_rsrc_t rw = __builtin_amdgcn_make_buffer_rsrc((void*)ws, 0, WS_END, 0x00020000);

    __amdgpu_buffer_rsrc_t ro = __builtin_amdgcn_make_buffer_rsrc((void*)out, 0, 2048 * 1280, 0x00020000);

    if (isR) {
        v4u xa = in4;
        u32 xb = in0;
        const u32 ep = in2, db = in3, wb = in5;
        {
            const v4u sent = {0xffffffffu, 0xffffffffu, 0xffffffffu, 0xffffffffu};
            const u32 n2 = 2 * w + ((lane >> 3) & 1);
            STW4(sent, WS_YG + (((tid >> 3) & 31) * 640u + ch0 + odd + (w >> 2)) * 128u + (lane & 7) * 16u);
            STW4(sent, lane < 16 ? WS_YG + ((n2 + (odd ? 8 : (w >> 2) * 16)) * 640u + ch0 + 2 - 2 * odd) * 128u + (lane & 7) * 16u : WS_OOB);
        }
        __builtin_amdgcn_s_setprio(3);
        SB();
        u32 sa = L_STG + w * STG1_RS + lane * 16, sb = L_STG + w * STG1_RS + 1024 + lane * 4;
        u32 ta = L_STG + (4 * (lane & 1) + ((lane >> 2) & 3)) * STG1_RS + (2 * (lane >> 4) + ((lane >> 1) & 1)) * 8 + w * 64;
        u32 uo = WS_UG + (u32)cw * 10240u + w * 512 + (lane >> 3) * 64 + (lane & 3) * 16 + ((lane >> 2) & 1) * 8;
        u32 uo2 = w < 4 ? uo + 8192 : WS_OOB;
        u32 gu[2], lu[2];
#pragma unroll
        for (int k = 0; k < 2; ++k) {
            const u32 i = tid + 512 * k;
            const u32 v = i / 3, s = i - 3 * v;
            gu[k] = WS_UG + v * 10240u + (ch0 + s) * 16u;
            lu[k] = L_U + s * U_CH + 480 + v * 16;
        }
        if (w >= 4) { gu[1] = WS_OOB; lu[1] = L_RED + lane * 16; }
        KEEP(gu[0]); KEEP(lu[0]); KEEP(gu[1]); KEEP(lu[1]);
        KEEP(sa); KEEP(sb); KEEP(ta); KEEP(uo); KEEP(uo2);
        u32 pfo, pq0 = 0;
        {
            const int e = (w & 1) * 64 + lane, r0 = e / 5;
            pfo = w < 4 ? (xj * 20 + min(r0, 19)) * 3456 + xc * 432 + min((e - r0 * 5) * 128, 428) : (xj * 54 + min(lane, 53)) * 1280 + xc * 160 + (w & 1) * 156;
            KEEP(pfo);
        }
        SB();
        PIN(xa); PIN(xb);
        TS(1);
        v2f xv[5];
        xv[0] = unpk(xa.x); xv[1] = unpk(xa.y); xv[2] = unpk(xa.z); xv[3] = unpk(xa.w); xv[4] = unpk(xb);
        v2f uv[5];
        {
            const float piv = rfl(xv[0].x);
            v2f s2 = xv[0] - piv, q2 = s2 * s2;
#pragma unroll
            for (int i = 1; i < 5; ++i) { v2f d = xv[i] - piv; s2 += d; q2 = __builtin_elementwise_fma(d, d, q2); }
            float s = s2.x + s2.y, q = q2.x + q2.y;
            wave_sum2(s, q);
            const float md = s * (1.0f / 640.0f);
            const float m = rbf(piv + md);
            const float var = rbf(fmaxf(fmaf(-md, md, q * (1.0f / 640.0f)), 0.f));
            const float den = rbf(__builtin_amdgcn_sqrtf(rbf(var + 1e-5f)));
            const float rden = __builtin_amdgcn_rcpf(den) * 0.0625f;
            u32 uh[5];
#pragma unroll
            for (int i = 0; i < 5; ++i) {
                uv[i] = unpk(pkbf2(unpk(pkbf2(xv[i] - m)) * rden));
                uh[i] = pkh2(uv[i]);
            }
            v4u u4 = {uh[0], uh[1], uh[2], uh[3]};
            LDS128(sa) = u4;
            LDS32(sb) = uh[4];
            TP(22);
            lds_arrive(L_CNTR);
        }
        const u32 tag = (u32)__builtin_amdgcn_readfirstlane((int)ep) * 0x9E3779B1u + 0x7F4A7C15u;
        LDS32(L_TAG) = tag;
        const v4u TB = {tag & TAGM, (tag >> 3) & TAGM, (tag << 2) & TAGM, (tag >> 6) & TAGM};
        {
            u32 t0 = (lane & 4) ? TB.z : TB.x, t1_ = (lane & 4) ? TB.w : TB.y;
            KEEP(t0); KEEP(t1_);
            __builtin_amdgcn_s_setprio(1);
            lds_wait<0>(L_CNTR, 8);
            TS(2);
            v2u r0 = __builtin_bit_cast(v2u, __builtin_amdgcn_ds_read_tr16_b64_v4i16((LAS v4s*)(uintptr_t)(ta)));
            v2u r1 = __builtin_bit_cast(v2u, __builtin_amdgcn_ds_read_tr16_b64_v4i16((LAS v4s*)(uintptr_t)(ta + 512)));
            v2u r2 = __builtin_bit_cast(v2u, __builtin_amdgcn_ds_read_tr16_b64_v4i16((LAS v4s*)(uintptr_t)(ta + 1024)));
            v2u o0 = {(r0.x & ~TAGM) | t0, (r0.y & ~TAGM) | t1_}, o1 = {(r1.x & ~TAGM) | t0, (r1.y & ~TAGM) | t1_}, o2 = {(r2.x & ~TAGM) | t0, (r2.y & ~TAGM) | t1_};
            STW2(o0, uo);
            __builtin_amdgcn_raw_buffer_store_b64(o1, rw, uo, 4096, 16);
            STW2(o2, uo2);
        }
        TS(3);
        SB();
        {
            sleep_n<D_U>();
            v4u a0 = LDW4(gu[0]), a1 = LDW4(gu[1]);
            gather2_fin<0>(rw, TB, gu[0], gu[1], a0, a1);
            LDS128(lu[0]) = a0; LDS128(lu[1]) = a1;
            asm volatile("s_waitcnt vmcnt(0)" :: "v"(in1) : "memory");
            SB();
            {
                const u16* pb = w < 2 ? wg : w < 4 ? wu : wd;
                __amdgpu_buffer_rsrc_t rp = __builtin_amdgcn_make_buffer_rsrc((void*)pb, 0, w < 6 ? 640 * 1728 * 2 : 0, 0x00020000);
                pq0 = __builtin_amdgcn_raw_buffer_load_b32(rp, pfo, 0, 0);
            }
            SB();
        }
        __builtin_amdgcn_s_setprio(3);
        TS(4);
        BAR();
        TP(24);
        __builtin_amdgcn_raw_buffer_store_b32(tag, rw, tid == 0 ? WS_PFL + (cw >> 2) * 128 + (cw & 3) * 4 : WS_OOB, 0, 16);
        SB();
        const v4u da = *(const GAS v4u*)(dsk + lane * 8);
        const v4u wa = *(const GAS v4u*)(wr + lane * 8);
        v4u wq[16];
        const int hf = (lane ^ (lane >> 1)) & 1;
        const int grp0 = w * 32 + (lane >> 1);
        const __amdgpu_buffer_rsrc_t rq = __builtin_amdgcn_make_buffer_rsrc((void*)(is_gu ? ((w & 1) ? wu : wg) : wd), 0, w < 4 ? 640 * 1728 * 2 : 0, 0x00020000);
        const int wst = is_gu ? 1728 : 640;
        u32 wo0, wo1, wo2;
        if (is_gu) {
            wo0 = (((w >> 1) ? 8 * lane + 6 : 8 * lane) * 1728 + fg * 8) * 2;
            wo1 = (((w >> 1) ? 512 + 2 * lane : 8 * lane + 2) * 1728 + fg * 8) * 2;
            wo2 = w < 2 ? wo1 + 2 * 3456 : WS_OOB;
        } else {
            wo0 = ((grp0 * 8 + 4 * hf) * 640 + dg * 16 + hf * 8) * 2;
            wo1 = wo0 + 2 * 1280;
            wo2 = wo0 + (4 - 8 * hf) * 1280;
        }
        u32 gy0 = WS_YG + ((u32)((cw >> 4) * 2 + (cw & 1)) * 640u + tid) * 128u + ((cw >> 1) & 7) * 16u;
        u32 gy1 = w < 2 ? gy0 + 512u * 128u : WS_OOB;
        u32 ly = L_STG + tid * 4;
        u32 yra = L_STG + (w >> 1) * STG2_RS + lane * 32, yrb = L_STG + (w >> 1) * STG2_RS + 2048 + lane * 8;
        KEEP(gy0); KEEP(gy1); KEEP(ly); KEEP(yra); KEEP(yrb);
        SB();
        if (w < 4) {
            sleep_n<D_W>();
            if (is_gu) {
                for (int i = 0; i < w; ++i) sleep_n<PACE / 4>();
                wq[2] = __builtin_amdgcn_raw_buffer_load_b128(rq, wo1, 0, 0);       sleep_n<PACE>();
                wq[3] = __builtin_amdgcn_raw_buffer_load_b128(rq, wo1, 3456, 0);    sleep_n<PACE>();
                wq[0] = __builtin_amdgcn_raw_buffer_load_b128(rq, wo0, 0, 0);       sleep_n<PACE>();
                wq[1] = __builtin_amdgcn_raw_buffer_load_b128(rq, wo0, 3456, 0);
            } else {
                for (int i = 0; i < w; ++i) sleep_n<PACE / 8>();
#pragma unroll
                for (int i = 0; i < 4; ++i) { wq[i] = __builtin_amdgcn_raw_buffer_load_b128(rq, wo0, i * 1280, 0); sleep_n<PACE / 2>(); }
                wq[6] = __builtin_amdgcn_raw_buffer_load_b128(rq, wo2, 2 * 1280, 0); sleep_n<PACE / 2>();
                wq[7] = __builtin_amdgcn_raw_buffer_load_b128(rq, wo2, 3 * 1280, 0);
            }
        }
        if (w == 7) {
            sleep_n<D_PFL>();
#pragma unroll 1
            for (int s = 0; s < SPIN_MAX; ++s) {
                const v4u f = LDW4(WS_PFL + lane * 128);
                if (__all(f.x == tag && f.y == tag && f.z == tag && f.w == tag)) break;
                __builtin_amdgcn_s_sleep(6);
                CBAR();
            }
        }
        BAR();
        asm volatile("" :: "v"(pq0));
        TS(5);
        SB();
        sleep_n<D_Y>();
        v4u ya0 = LDW4(gy0), ya1 = LDW4(gy1);
        SB();
        v2f t1[5], wv[5];
        float wsum;
        {
            const u32 dpk[5] = {da.x, da.y, da.z, da.w, db};
            const u32 wp[5] = {wa.x, wa.y, wa.z, wa.w, wb};
            v2f s2 = {0.f, 0.f};
#pragma unroll
            for (int i = 0; i < 5; ++i) {
                t1[i] = unpk(pkbf2((uv[i] * 16.0f) * unpk(dpk[i])));
                wv[i] = unpk(wp[i]); s2 += wv[i];
                KEEP(t1[i]); KEEP(wv[i]);
            }
            wsum = wave_sum(s2.x + s2.y);
        }
        SB();
        {
            gather2_fin<1>(rw, TB, gy0, gy1, ya0, ya1);
            LDS32(ly) = ya0.x; LDS32(ly + STG2_RS) = ya0.y; LDS32(ly + 2 * STG2_RS) = ya0.z; LDS32(ly + 3 * STG2_RS) = ya0.w;
            if (w < 2) { LDS32(ly + 2048) = ya1.x; LDS32(ly + 2048 + STG2_RS) = ya1.y; LDS32(ly + 2048 + 2 * STG2_RS) = ya1.z; LDS32(ly + 2048 + 3 * STG2_RS) = ya1.w; }
            SB();
            wq[4] = __builtin_amdgcn_raw_buffer_load_b128(rq, wo2, 0, 0);
            wq[5] = __builtin_amdgcn_raw_buffer_load_b128(rq, wo2, wst * 2, 0);
            SB();
        }
        TS(6);
        BAR();
        v2f hv[5];
        u32 hp[5];
        float m2, rden2;
        {
            const v4u y0 = LDS128(yra), y1 = LDS128(yra + 16);
            const v2u y2 = LDS64(yrb);
            const u32 yd[10] = {y0.x, y0.y, y0.z, y0.w, y1.x, y1.y, y1.z, y1.w, y2.x, y2.y};
            const u32 sel = (w & 1) ? 0x03020c0cu : 0x01000c0cu;
#pragma unroll
            for (int i = 0; i < 5; ++i) {
                v2f yv = {__uint_as_float(__builtin_amdgcn_perm(0u, yd[2 * i], sel)), __uint_as_float(__builtin_amdgcn_perm(0u, yd[2 * i + 1], sel))};
                hp[i] = pkbf2(xv[i] + unpk(pkbf2(yv + t1[i])));
                hv[i] = unpk(hp[i]);
            }
            const float piv = rfl(hv[0].x);
            v2f s2 = hv[0] - piv, q2 = s2 * s2, r2 = s2 * wv[0];
#pragma unroll
            for (int i = 1; i < 5; ++i) {
                v2f d = hv[i] - piv; s2 += d; q2 = __builtin_elementwise_fma(d, d, q2); r2 = __builtin_elementwise_fma(d, wv[i], r2);
            }
            float s = s2.x + s2.y, q = q2.x + q2.y, rs = r2.x + r2.y;
            wave_sum3(s, q, rs);
            const float md = s * (1.0f / 640.0f);
            m2 = rbf(piv + md);
            const float var = rbf(fmaxf(fmaf(-md, md, q * (1.0f / 640.0f)), 0.f));
            const float den = rbf(__builtin_amdgcn_sqrtf(rbf(var + 1e-5f)));
            rden2 = __builtin_amdgcn_rcpf(den);
            const float r = (rs - (m2 - piv) * wsum) * rden2;
            const int rb = (int)(pkbf(r, r) & 0xffff0000u);
            const u32 ord = (u32)rb ^ ((u32)(rb >> 31) | 0x80000000u);
            LDS32(L_ROWKEY + w * 4) = (ord & 0xffff0000u) | (u32)(2047 - row);
        }
        TS(7);
        BAR();
        u32 op[5];
        bool mine;
        {
            const v4u k0 = LDS128(L_ROWKEY), k1 = LDS128(L_ROWKEY + 16);
            const u32 kb = max(max(max(k0.x, k0.y), max(k0.z, k0.w)), max(max(k1.x, k1.y), max(k1.z, k1.w)));
            { v2u o = {kb, tag}; STW2(o, tid == 0 ? WS_KEY + (u32)cw * 128u : WS_OOB); }
            __builtin_amdgcn_s_setprio(1);
            SB();
            u32 xnp[5];
#pragma unroll
            for (int i = 0; i < 5; ++i) {
                xnp[i] = pkbf2(unpk(pkbf2(hv[i] - m2)) * rden2);
                op[i] = pkbf2(hv[i] + unpk(xnp[i]));
            }
            mine = (2047 - (int)(__builtin_amdgcn_readfirstlane((int)kb) & 0xffff)) == row;
            if (mine) {
                const u32 xo = WS_XS + (u32)cw * 5120u + lane * 16;
#pragma unroll
                for (int i = 0; i < 5; ++i) { v4u o = {xnp[i], tag, hp[i], tag}; STW4(o, xo + i * 1024); }
            } else {
                v4u o4 = {op[0], op[1], op[2], op[3]};
                __builtin_amdgcn_raw_buffer_store_b128(o4, ro, row * 1280 + lane * 16, 0, OUT_AUX);
                __builtin_amdgcn_raw_buffer_store_b32(op[4], ro, row * 1280 + 1024 + lane * 4, 0, OUT_AUX);
            }
        }
        const bool r1ok = grp0 + 128 < 216;
        u32 wp[64];
        u32 rad = L_RED + (((is_gu ? (w & 1) : hf) * 8 + ((lane >> 1) & 7)) * 16 + (is_gu ? (w >> 1) * 8 + (lane >> 4) * 2 + (lane & 1) : w * 4 + (lane >> 4))) * 4;
        if (w < 4) {
#pragma unroll
            for (int rp = 0; rp < 4; ++rp) {
#pragma unroll
                for (int j = 0; j < 4; ++j) {
                    wp[8 * rp + 2 * j] = __builtin_amdgcn_perm(wq[2 * rp + 1][j], wq[2 * rp][j], 0x05040100u);
                    wp[8 * rp + 2 * j + 1] = __builtin_amdgcn_perm(wq[2 * rp + 1][j], wq[2 * rp][j], 0x07060302u);
                    KEEP(wp[8 * rp + 2 * j]); KEEP(wp[8 * rp + 2 * j + 1]);
                }
            }
            SB();
            if (!is_gu) {
                const u32 v0 = r1ok ? wo0 + 128 * 8 * 1280 : WS_OOB, v2 = r1ok ? wo2 + 128 * 8 * 1280 : WS_OOB;
#pragma unroll
                for (int i = 0; i < 4; ++i) {
                    wq[8 + i] = __builtin_amdgcn_raw_buffer_load_b128(rq, v0, i * 1280, 0);
                    wq[12 + i] = __builtin_amdgcn_raw_buffer_load_b128(rq, v2, i * 1280, 0);
                }
            }
        }
        KEEP(rad);
        TS(8);
        BAR();
        u32 kbest;
        {
            const v4u k0 = LDS128(L_WKEY);
            kbest = (u32)__builtin_amdgcn_readfirstlane((int)max(max(k0.x, k0.y), max(k0.z, k0.w)));
        }
        const int idx = 2047 - (int)(kbest & 0xffffu);
        const u32 xs = WS_XS + (u32)(idx >> 3) * 5120u;
        v4u fin = {0u, 0u, 0u, 0u};
        if (is_gu && w < 4) {
            const u32 xo = xs + lane * 16 + (w >> 1) * 3072;
            v4u a0, a1, a2;
            gather3(rw, tag, xo, xo + 1024, w < 2 ? xo + 2048 : WS_OOB, a0, a1, a2);
            TS(9);
            float acc[8];
#pragma unroll
            for (int j = 0; j < 8; ++j) acc[j] = dot2bf(a1.x, wp[8 + j], dot2bf(a0.x, wp[j], 0.f));
            if (w < 2) {
#pragma unroll
                for (int j = 0; j < 8; ++j) acc[j] = dot2bf(a2.x, wp[16 + j], acc[j]);
            }
            LDSF(rad) = rowreduce8(acc, lane);
        }
        if (mine && idx != row) {
            v4u o4 = {op[0], op[1], op[2], op[3]};
            __builtin_amdgcn_raw_buffer_store_b128(o4, ro, row * 1280 + lane * 16, 0, OUT_AUX);
            __builtin_amdgcn_raw_buffer_store_b32(op[4], ro, row * 1280 + 1024 + lane * 4, 0, OUT_AUX);
        }
        if (w < 4) {
            float rsel = 0.f;
            if (!is_gu) {
#pragma unroll
                for (int rp = 4; rp < 8; ++rp) {
#pragma unroll
                    for (int j = 0; j < 4; ++j) {
                        wp[8 * rp + 2 * j] = __builtin_amdgcn_perm(wq[2 * rp + 1][j], wq[2 * rp][j], 0x05040100u);
                        wp[8 * rp + 2 * j + 1] = __builtin_amdgcn_perm(wq[2 * rp + 1][j], wq[2 * rp][j], 0x07060302u);
                        KEEP(wp[8 * rp + 2 * j]); KEEP(wp[8 * rp + 2 * j + 1]);
                    }
                }
                if (w == 0) {
                    const int pr = 8 * dg + (lane >> 3);
                    const int sl = pr < 256 ? 64 * (pr & 3) + (pr >> 2) : pr;
                    fin = gather1(rw, tag, xs + sl * 16);
                    const u32 rb = (kbest & 0x80000000u) ? (kbest & 0x7fff0000u) : (~kbest & 0xffff0000u);
                    rsel = rbf(__builtin_amdgcn_rcpf(1.0f + __expf(-__uint_as_float(rb))));
                    KEEP(rsel);
                    sleep_n<(D_ACT > 12 ? D_ACT - 12 : 0)>();
                } else {
                    sleep_n<D_ACT>();
                }
                const u32 ao = WS_ACT + (u32)grp0 * 128u + hf * 16u;
                v4u a0, a1, a2;
                gather3(rw, tag, ao, r1ok ? ao + 128u * 128u : WS_OOB, WS_OOB, a0, a1, a2);
                TS(9);
                const u32 b0 = dppu0<0xB1, 0xf, 0xf>(a0.x), b1 = dppu0<0xB1, 0xf, 0xf>(a0.z);
                const u32 c0 = dppu0<0xB1, 0xf, 0xf>(a1.x), c1 = dppu0<0xB1, 0xf, 0xf>(a1.z);
                float acc[8];
#pragma unroll
                for (int j = 0; j < 8; ++j) {
                    float v = dot2bf(b1, wp[24 + j], dot2bf(b0, wp[16 + j], dot2bf(a0.z, wp[8 + j], dot2bf(a0.x, wp[j], 0.f))));
                    acc[j] = dot2bf(c1, wp[56 + j], dot2bf(c0, wp[48 + j], dot2bf(a1.z, wp[40 + j], dot2bf(a1.x, wp[32 + j], v))));
                }
                LDSF(rad) = rowreduce8(acc, lane);
            }
            BAR();
            TS(10);
            if (w == 0) {
                const v4f pp = LDSF4(L_RED + lane * 16);
                float s = (pp[0] + pp[1]) + (pp[2] + pp[3]);
                s += dpp0<0xB1, 0xf, 0xf>(s);
                s += dpp0<0x4E, 0xf, 0xf>(s);
                s = rbf(s);
                if (is_gu) {
                    const auto gu2 = __builtin_amdgcn_permlane32_swap(__float_as_uint(s), __float_as_uint(s), false, false);
                    const float g = __uint_as_float(gu2[0]), u = __uint_as_float(gu2[1]);
                    const float sg = rbf(g * __builtin_amdgcn_rcpf(1.0f + __expf(-g)));
                    const float act = sg * u;
                    const u32 pk = pkbf(act, dpp0<0x104, 0xf, 0xf>(act));
                    const u32 pk2 = dppu0<0x108, 0xf, 0xf>(pk);
                    if (lane == 0 || lane == 16) { v4u o = {pk, tag, pk2, tag}; STW4(o, WS_ACT + (u32)fg * 128u + lane); }
                } else {
                    const float ya = rbf(s * rsel);
                    const float yb = dpp0<0x104, 0xf, 0xf>(ya);
                    const u32 bp = pkbf(lo2f(fin.x) + ya, hi2f(fin.x) + yb);
                    const u32 o = pkbf(lo2f(fin.z) + lo2f(bp), hi2f(fin.z) + hi2f(bp));
                    if ((lane & 7) == 0) __builtin_amdgcn_raw_buffer_store_b32(o, ro, idx * 1280 + dg * 32 + (lane >> 1), 0, OUT_AUX);
                }
            }
        }
        TS(11);
    } else {
        const int t = tid - 512;
        const bool doX = k < 6, doY = k < 4 || k >= 6;
        const v4u kt3 = in4;
        u32 ldt = in1, la2 = in0, ai2 = in2, cr2 = in3, ci2 = in5;
        v4u bx, by, kt2;
        const v4u z4u = {0u, 0u, 0u, 0u};
        asm volatile("s_mov_b32 m0, %0" :: "s"(L_TAB - 32768 + k * TAB_W) : "memory");
        const int cslot = k < 4 ? odd + (k & 1) : (odd || k == 5) ? 0 : 2;
        u32 tr = L_TAB + k * TAB_W + (lane & 15) * TAB_RS + (lane & 48);
        u32 xa_ = L_XCH + (k >> 1) * 3072 + lane * 16;
        const u32 cnt = L_CNTK + (k >> 1) * 12;
        KEEP(tr); KEEP(xa_);
        SB();
        {
            PIN(ldt); PIN(la2); PIN(ai2); PIN(cr2); PIN(ci2);
            TS(1); TC(8);
            const u32 sel = hh ? 0x03020c0cu : 0x01000c0cu;
#define MYH(v) __uint_as_float(__builtin_amdgcn_perm(0u, (v), sel))
            const v2f dA = unpk(pkbf(exp_bf(lo2f(ldt)), exp_bf(MYH(la2))));
            const float dt = dA.x;
            const float lam = (-dA.y * dt) * 1.44269504088896341f;
            const float om = (MYH(ai2) * dt) * 0.15915494309189535f;
#define TO(rowi) (32768 + (rowi) * TAB_RS)
#define TW4(r, a, b, c, d) tw4<TO(r), TO((r) + 1), TO((r) + 2), TO((r) + 3)>(a, b, c, d)
#define TW3(r, a, b, c) tw3<TO(r), TO((r) + 1), TO((r) + 2)>(a, b, c)
            const v2f z8 = zpow(lam, om, 8.0f);
            v4u Bf[4];
            {
                const v2f one = {1.0f, 0.0f};
                const v2f c1 = {z8.x, -z8.y};
                const v2f c2 = cmul(c1, c1), c3 = cmul(c2, c1), c4 = cmul(c2, c2), c5 = cmul(c4, c1), c6 = cmul(c4, c2), c7 = cmul(c4, c3);
                const v2f c8 = cmul(c4, c4);
                const v2f c9 = cmul(c8, c1), c10 = cmul(c8, c2), c11 = cmul(c8, c3), c12 = cmul(c8, c4), c13 = cmul(c8, c5), c14 = cmul(c8, c6), c15 = cmul(c8, c7);
                TW4(0, one, c1, c2, c3); TW4(4, c4, c5, c6, c7); TW4(8, c8, c9, c10, c11); TW4(12, c12, c13, c14, c15);
#pragma unroll
                for (int s = 0; s < 4; ++s) Bf[s] = LDS128(tr + s * 64);
            }
            const u32 cm = max(cr2 & 0x7fff7fffu, ci2 & 0x7fff7fffu), cl = max((cr2 << 16) & 0x7fff0000u, (ci2 << 16) & 0x7fff0000u);
            const int ec = (int)(wave_maxu(max(cm, cl)) >> 23), ed = __builtin_amdgcn_readfirstlane((int)(__float_as_uint(dt) >> 23)) & 0xff;
            const int e = min(max(15 - 11 - (ec - 127) - (ed - 127), -100), 100);
            const float S2 = __uint_as_float((u32)(128 + e) << 23);
            LDS32(L_INVS + slot * 4) = (u32)(127 + 4 - e) << 23;
            const u32 ea = kt3.x + slot * KC_CH, qa = kt3.y + slot * KC_CH, ka = kt3.z + slot * KC_CH;
            kt2 = *(const GAS v4u*)(ktb + 2048 + lane * 16);
            bx = *(const GAS v4u*)(ktb + lane * 16); by = *(const GAS v4u*)(ktb + 1024 + lane * 16);
            if (hh ? doY : doX) LDS128(kt3.w + slot * (hh ? (u32)KC_CH : kt3.z)) = z4u;
            v2f G0 = {MYH(cr2), MYH(ci2)};
            G0 = (G0 * dt) * S2;
            const v2f z1 = zpow(lam, om, 1.0f);
            const v2f z128 = zpow(lam, om, 128.0f);
            const v2f z2 = cmul(z1, z1), z4 = cmul(z2, z2), z6 = cmul(z4, z2);
            const v2f G1 = cmul(G0, z128);
            const v4f zero4 = {0.f, 0.f, 0.f, 0.f};
#define LDS16(off) (*(LAS u16*)(uintptr_t)(off))
            auto tile_mma = [&](int ti, bool keep) -> v4f {
                v4u Af[4];
#pragma unroll
                for (int s = 0; s < 4; ++s) Af[s] = LDS128(tr + s * 64);
                v4f d = zero4;
                if (keep) { lds_wait<1>(cnt + ti * 4, 1); d = LDSF4(xa_ + ti * 1024); }
#pragma unroll
                for (int s = 0; s < 4; ++s) d = mfma16(Af[s], Bf[s], d);
                if (!keep) { LDSF4(xa_ + ti * 1024) = d; lds_arrive(cnt + ti * 4); }
                return d;
            };
            auto scat4 = [&](u32 o, u32 p0, u32 p1) {
                const u32 mid = __builtin_amdgcn_alignbyte(p1, p0, 2);
                v2u p01 = {p0, p1};
                LDS64(o) = p01;
                LDS16(o + KC_CP - 2) = (u16)p0; LDS32(o + KC_CP) = mid; LDS16(o + KC_CP + 4) = (u16)(p1 >> 16);
                LDS32(o + 2 * KC_CP - 4) = p0; LDS32(o + 2 * KC_CP) = p1;
                LDS16(o + 3 * KC_CP - 6) = (u16)p0; LDS32(o + 3 * KC_CP - 4) = mid; LDS16(o + 3 * KC_CP) = (u16)(p1 >> 16);
            };
            v2f G2, G3, G4, G5, G6, G7, g78, Ha, Hb;
            if (doY) {
                G2 = cmul(G1, z128); G3 = cmul(G2, z128); G4 = cmul(G3, z128); G5 = cmul(G4, z128); G6 = cmul(G5, z128); G7 = cmul(G6, z128);
                g78 = cmul(G7, z8);
                Ha = g78 * 0.5f;
                const v2f z256 = cmul(z128, z128);
                Hb = cmul(Ha, cmul(z256, z256));
            }
            auto tile2 = [&](bool keep) -> v4f {
                const v2f h0 = cmul(Ha, z128), a4 = cmul(G4, z4), a8 = cmul(G4, z8);
                const v2f h1 = cmul(h0, z128), b4 = cmul(G5, z4), b8 = cmul(G5, z8);
                TW4(0, G4, a4, a8, h0); TW4(4, G5, b4, b8, h1);
                const v2f h2 = cmul(h1, z128), c4 = cmul(G6, z4), c8 = cmul(G6, z8);
                const v2f h3 = cmul(h2, z128), d4 = cmul(G7, z4);
                TW4(8, G6, c4, c8, h2); TW4(12, G7, d4, g78, h3);
                return tile_mma(2, keep);
            };
            if (doY && hh == 0) tile2(false);
            if (doX) {
                const v2f z3 = cmul(z2, z1), z5 = cmul(z4, z1), z7 = cmul(z6, z1);
                sfor<2>([&](auto ia) {
                    constexpr int a = ia.value;
                    const v2f G = a ? G1 : G0;
                    const v2f g1 = cmul(G, z1), g2 = cmul(G, z2), g3 = cmul(G, z3), g4 = cmul(G, z4), g5 = cmul(G, z5), g6 = cmul(G, z6), g7 = cmul(G, z7);
                    TW4(8 * a, G, g1, g2, g3); TW4(8 * a + 4, g4, g5, g6, g7);
                });
                const v4f d0 = tile_mma(0, hh == 0);
                if (hh == 0) scat4(ea + 512 + 8, pkh(d0[3], d0[2]), pkh(d0[1], d0[0]));
            }
            if (doY) {
                const v2f h0 = cmul(Hb, z128), a2 = cmul(G2, z2), a4 = cmul(G2, z4);
                const v2f h1 = cmul(h0, z128), a6 = cmul(G2, z6), a8 = cmul(G2, z8);
                TW4(0, G2, a2, a4, h0); TW4(4, a4, a6, a8, h1);
                const v2f h2 = cmul(h1, z128), b2 = cmul(G3, z2), b4 = cmul(G3, z4);
                const v2f h3 = cmul(h2, z128), b6 = cmul(G3, z6), b8 = cmul(G3, z8);
                TW4(8, G3, b2, b4, h2); TW4(12, b4, b6, b8, h3);
                const v4f d1 = tile_mma(1, hh == 0);
                TS(2); TC(9);
                if (hh == 0) {
                    scat4(ea + 8, pkh(d1[2], d1[1]), pkh(d1[0], d1[0]));
                    const u16 hq = (u16)pkh(d1[3], d1[3]);
                    LDS16(qa) = hq; LDS16(qa + 18) = hq; LDS16(qa + 36) = hq; LDS16(qa + 54) = hq;
                } else {
                    const v4f d2 = tile2(true);
                    const u32 p22 = pkh(d2[2], d2[2]), p11 = pkh(d2[1], d2[1]), p10 = pkh(d2[1], d2[0]), p00 = pkh(d2[0], d2[0]), p21 = pkh(d2[2], d2[1]);
                    const u32 o = ea + 8;
                    v4u q4 = {p22, p11, p10, p00};
                    v2u qa2 = {p21, p11}, qb2 = {p11, p10}, qc2 = {p11, p00};
                    LDS128(o) = q4;
                    LDS16(o + KC_CP - 2) = (u16)p22; LDS64(o + KC_CP) = qa2; LDS32(o + KC_CP + 8) = p00; LDS16(o + KC_CP + 12) = (u16)p00;
                    LDS32(o + 2 * KC_CP - 4) = p22; LDS64(o + 2 * KC_CP) = qb2; LDS32(o + 2 * KC_CP + 8) = p00;
                    LDS16(o + 3 * KC_CP - 6) = (u16)p22; LDS32(o + 3 * KC_CP - 4) = p21; LDS64(o + 3 * KC_CP) = qc2; LDS16(o + 3 * KC_CP + 8) = (u16)p00;
                    const u16 hq = (u16)pkh(d2[3], d2[3]);
                    LDS16(qa) = hq; LDS16(qa + 18) = hq; LDS16(qa + 36) = hq; LDS16(qa + 54) = hq;
                    LDS16(ka) = (u16)__builtin_amdgcn_readlane((int)p22, 63);
                }
            }
        }
        const u32 invad = L_INVS + cslot * 4;
        u32 pq[6] = {0, 0, 0, 0, 0, 0};
        u32 aad = kt2.x + cslot * KC_CH, bad = kt2.y + cslot * U_CH, qad = kt2.z + cslot * KC_CH, yoff = kt2.w + (ch0 + cslot) * 128;
        KEEP(aad); KEEP(bad); KEEP(qad); KEEP(yoff); KEEP(bx); KEEP(by);
        __builtin_amdgcn_s_setprio(2);
        TS(3);
        BAR();
        TS(4); TC(10);
        if (k == 2 || k == 3) conv_job<7, 4, 1>(rw, invad, aad, bad, qad, yoff, bx, by TSPASS);
        else if (k == 4) { if (odd) conv_job<5, 2, 1>(rw, invad, aad, bad, qad, yoff, bx, by TSPASS); else conv_job<7, 2, 1>(rw, invad, aad, bad, qad, yoff, bx, by TSPASS); }
        else if (k == 1 || (k == 0 && odd)) conv_job<3, 4, 1>(rw, invad, aad, bad, qad, yoff, bx, by TSPASS);
        else if (k == 0 || (k == 5 && odd)) conv_job<3, 2, 1>(rw, invad, aad, bad, qad, yoff, bx, by TSPASS);
        else if (k == 5) conv_job<1, 2, 2>(rw, invad, aad, bad, qad, yoff, bx, by TSPASS);
        TS(5);
        BAR();
        const u32 tag = (u32)__builtin_amdgcn_readfirstlane((int)LDS32(L_TAG));
        BAR();
        BAR();
        TS(6);
        if (k >= 4) {
            sleep_n<D_KEY>();
            const u32 kv = gather1h(rw, tag, WS_KEY + (u32)(t - 256) * 128u);
            const u32 km = wave_maxu(kv);
            LDS32(L_WKEY + (k - 4) * 4) = km;
        }
        TS(7);
        BAR();
        if (b == 0 && t == 0) __builtin_amdgcn_raw_buffer_store_b32(tag, rw, WS_EPOCH, 0, 16);
        asm volatile("" :: "v"(pf));
        asm volatile("" :: "v"(pq[0]), "v"(pq[1]), "v"(pq[2]), "v"(pq[3]), "v"(pq[4]), "v"(pq[5]));
    }
}

extern "C" void kernel_launch(void* const* d_in, const int* in_sizes, int n_in, void* d_out, int out_size,
                              void* d_ws, size_t ws_size, hipStream_t stream) {
    (void)in_sizes; (void)n_in; (void)out_size; (void)ws_size;
    fused_s4d_mod<<<256, 1024, 0, stream>>>(
        (const u16*)d_in[0], (const u16*)d_in[1], (const u16*)d_in[2], (const u16*)d_in[3], (const u16*)d_in[4],
        (const u16*)d_in[5], (const u16*)d_in[6], (const u16*)d_in[8], (const u16*)d_in[9], (const u16*)d_in[10],
        (const u16*)d_in[11], (u16*)d_out, (char*)d_ws);
}
```
